```python
import torch

def _softmax(x, axis=-1):
    return torch.softmax(x, dim=axis)
def _gelu(x):
    return torch.nn.functional.gelu(x, approximate="tanh")
def _silu(x):
    return torch.nn.functional.silu(x)
def _sigmoid(x):
    return torch.sigmoid(x)
def _layernorm(x, axis=-1, eps=1e-5):
    m = x.mean(dim=axis, keepdim=True)
    v = x.var(dim=axis, keepdim=True, unbiased=False)
    return (x - m) / torch.sqrt(v + eps)
def _scatter_add(tgt, idx, src, axis=0):
    return tgt.clone().index_add_(axis, idx, src)
def _segment_sum(src, seg, n):
    out = torch.zeros((n,) + src.shape[1:], dtype=src.dtype)
    return out.index_add_(0, seg, src)
def _pad(x, pw):
    flat = tuple(v for pair in reversed(pw) for v in pair)
    return torch.nn.functional.pad(x, flat)

kernel_name = 'parallel_sum__mha_block_cached__mixer_channel_mlp__decode'

def setup_inputs():

    _w = (lambda a: (lambda t: t.to(torch.bfloat16) if t.is_floating_point() else t)(torch.as_tensor(a)))
    def _A_setup():
        B, Sq, D = 8, 1, 1024
        H, Sk, Dh = 32, 4096, 128
        return {
            "x": _w((lambda *s: torch.randn(*s, dtype=torch.bfloat16) * 0.05)(B, Sq, D)),
            "k_cache": _w((lambda *s: torch.randn(*s, dtype=torch.bfloat16) * 3.2)(B, H, Sk, Dh)),
            "v_cache": _w((lambda *s: torch.randn(*s, dtype=torch.bfloat16) * 0.4)(B, H, Sk, Dh)),
            "wq": _w((lambda *s: torch.randn(*s, dtype=torch.bfloat16) * 3.2)(D, H * Dh) * (D ** -0.5)),
            "wo": _w((lambda *s: torch.randn(*s, dtype=torch.bfloat16) * 0.4)(H * Dh, D) * ((H * Dh) ** -0.5)),
        }
    _A_in = _A_setup()

    def _B_setup():
        B, S, D, F = 8, 1, 1024, 3584
        return {
            "x": _w((lambda *s: torch.randn(*s, dtype=torch.bfloat16) * 0.05)(B, S, D)),
            "w1": _w((lambda *s: torch.randn(*s, dtype=torch.bfloat16) * 3.2)(D, F) * (D ** -0.5)),
            "b1": _w((lambda *s: torch.randn(*s, dtype=torch.bfloat16) * 1.6)(F)),
            "w2": _w((lambda *s: torch.randn(*s, dtype=torch.bfloat16) * 0.1)(F, D) * (F ** -0.5)),
            "b2": _w((lambda *s: torch.randn(*s, dtype=torch.bfloat16) * 0.1)(D)),
        }
    _B_in = _B_setup()

    return {
        "A_x": _A_in["x"],
        "A_k_cache": _A_in["k_cache"],
        "A_v_cache": _A_in["v_cache"],
        "A_wq": _A_in["wq"],
        "A_wo": _A_in["wo"],
        "B_x": _B_in["x"],
        "B_w1": _B_in["w1"],
        "B_b1": _B_in["b1"],
        "B_w2": _B_in["w2"],
        "B_b2": _B_in["b2"],
    }

def reference(A_x, A_k_cache, A_v_cache, A_wq, A_wo, B_x, B_w1, B_b1, B_w2, B_b2):
    def _A_ref(x, k_cache, v_cache, wq, wo):
        B, Sq, D = x.shape
        H, Dh = 32, 128
        q = (lambda x, *d: x.permute(*d))((x @ wq).reshape(B, Sq, H, Dh), 0, 2, 1, 3)
        scores = torch.einsum("bhqd,bhkd->bhqk", q, k_cache) * (Dh ** -0.5)
        p = _softmax(scores, axis=-1)
        o = torch.einsum("bhqk,bhkd->bhqd", p, v_cache)
        return (lambda x, *d: x.permute(*d))(o, 0, 2, 1, 3).reshape(B, Sq, H * Dh) @ wo

    def _B_ref(x, w1, b1, w2, b2):
        y = _layernorm(x, axis=-1)
        h = _gelu(torch.einsum("bsd,df->bsf", y, w1) + b1)
        return torch.einsum("bsf,fd->bsd", h, w2) + b2

    x = A_x
    xn = _layernorm(x, axis=-1)
    return x + _A_ref(xn, A_k_cache, A_v_cache, A_wq, A_wo) + _B_ref(xn, B_w1, B_b1, B_w2, B_b2)
```

```cpp
#include <hip/hip_runtime.h>
#include <hip/hip_bf16.h>
#include <stdint.h>

#define NSW 4
#define NBUF 2
#define W_AUX 2
#define KV_AUX 2
#define MIMIC 1
#define G_NT 0
#define TAU 243
#define T0 1100u
#define TAUV 226
#define STAG (TAU / 32)
#define LSLEEP 8
#define W2 1
#define PRE 16
#define RESUME 4
#define NDELAY 24
#define SLEEP1 64
#define SLEEP2 32

typedef unsigned u32x4 __attribute__((ext_vector_type(4)));
typedef unsigned u32x2 __attribute__((ext_vector_type(2)));
typedef float f32x4 __attribute__((ext_vector_type(4)));
typedef float f32x2 __attribute__((ext_vector_type(2)));
typedef __bf16 bf16x2 __attribute__((ext_vector_type(2)));
typedef __bf16 bf16x8 __attribute__((ext_vector_type(8)));
typedef __attribute__((address_space(1))) unsigned gu32;
typedef __attribute__((address_space(1))) unsigned long long gu64;
typedef __attribute__((address_space(3))) void* lds_ptr_t;
typedef const __attribute__((address_space(1))) void* glb_ptr_t;

constexpr int NB_ = 8, NH = 32, SK = 4096, DH = 128, DM = 1024, FF = 3584, FS = FF / NH;
constexpr int NT = 64 / NSW;
constexpr int NS = (NT + NBUF - 1) / NBUF * NBUF;
constexpr int NTHR = (NSW + 4) * 64;
constexpr int AROW = 272;
constexpr int L_P = 0;
constexpr int L_MA = 20480;
constexpr int L_ML = L_MA + 4096;
constexpr int L_CNT = L_ML + 64;
constexpr int L_AH = L_CNT + 64;
constexpr int L_AO = L_AH + 16 * AROW;
constexpr int L_W2 = L_AO + 16 * AROW;
constexpr int L_WO = L_W2 + 32768;
constexpr int L_AX = L_WO + 32768;
constexpr int L_RED = L_AX + 32 * AROW;
constexpr int L_QS = L_RED + 16384;
constexpr int L_BM = L_QS + 2048;
constexpr int L_END = L_BM + 256;
constexpr unsigned WS_MSG = 0;
constexpr unsigned WS_OX = 1u << 20;
constexpr unsigned WS_HO = WS_OX + (128u << 10);
constexpr unsigned WS_QIN = WS_HO + (1u << 20);
constexpr unsigned WS_HEL = WS_QIN + (2u << 20);
constexpr unsigned WS_NON = WS_HEL + (128u << 10);
constexpr unsigned WS_HAND = WS_NON + (32u << 10);

#define RLX_AGENT __ATOMIC_RELAXED, __HIP_MEMORY_SCOPE_AGENT
#define RLX_WG __ATOMIC_RELAXED, __HIP_MEMORY_SCOPE_WORKGROUP

__device__ __forceinline__ float dot2(unsigned k, unsigned q, float acc) {
  return __builtin_amdgcn_fdot2_f32_bf16(__builtin_bit_cast(bf16x2, k), __builtin_bit_cast(bf16x2, q), acc, false);
}
template <int CTRL> __device__ __forceinline__ float dpp(float x) {
  return __builtin_bit_cast(float, __builtin_amdgcn_mov_dpp(__builtin_bit_cast(int, x), CTRL, 0xf, 0xf, true));
}
constexpr int XOR1 = 0xB1, XOR2 = 0x4E, XOR7 = 0x141, XOR8 = 0x128;
__device__ __forceinline__ float xrow16_sum(float x) {
  auto s = __builtin_amdgcn_permlane16_swap(__float_as_uint(x), __float_as_uint(x), false, false);
  x = __uint_as_float(s[0]) + __uint_as_float(s[1]);
  auto t = __builtin_amdgcn_permlane32_swap(__float_as_uint(x), __float_as_uint(x), false, false);
  return __uint_as_float(t[0]) + __uint_as_float(t[1]);
}
__device__ __forceinline__ float xrow16_max(float x) {
  auto s = __builtin_amdgcn_permlane16_swap(__float_as_uint(x), __float_as_uint(x), false, false);
  x = fmaxf(__uint_as_float(s[0]), __uint_as_float(s[1]));
  auto t = __builtin_amdgcn_permlane32_swap(__float_as_uint(x), __float_as_uint(x), false, false);
  return fmaxf(__uint_as_float(t[0]), __uint_as_float(t[1]));
}
__device__ __forceinline__ float wave_sum(float x) {
  x += dpp<XOR1>(x); x += dpp<XOR2>(x); x += dpp<XOR7>(x); x += dpp<XOR8>(x);
  return xrow16_sum(x);
}
__device__ __forceinline__ float wave_max(float x) {
  x = fmaxf(x, dpp<XOR1>(x)); x = fmaxf(x, dpp<XOR2>(x)); x = fmaxf(x, dpp<XOR7>(x)); x = fmaxf(x, dpp<XOR8>(x));
  return xrow16_max(x);
}
__device__ __forceinline__ float bf_lo(unsigned u) { return __builtin_bit_cast(float, u << 16); }
__device__ __forceinline__ float bf_hi(unsigned u) { return __builtin_bit_cast(float, u & 0xffff0000u); }
__device__ __forceinline__ unsigned pack_bf16(float lo, float hi) {
  bf16x2 v; v[0] = (__bf16)lo; v[1] = (__bf16)hi; return __builtin_bit_cast(unsigned, v);
}
__device__ __forceinline__ void fma8(f32x2 (&acc)[4], float s, u32x4 w) {
  asm volatile("" : "+v"(w));
  const f32x2 sv = {s, s};
#pragma unroll
  for (int k = 0; k < 4; ++k) { const f32x2 wv = {bf_lo(w[k]), bf_hi(w[k])}; acc[k] = __builtin_elementwise_fma(sv, wv, acc[k]); }
  asm volatile("" : "+v"(acc[0]), "+v"(acc[1]), "+v"(acc[2]), "+v"(acc[3]));
}
#define LDS_F(off) (reinterpret_cast<float*>(smem + (off)))
#define LDS_U(off) (reinterpret_cast<unsigned*>(smem + (off)))
#define WAIT_LGKM() asm volatile("s_waitcnt lgkmcnt(0)" ::: "memory")
#define WAIT_VM() asm volatile("s_waitcnt vmcnt(0)" ::: "memory")
#define BARRIER() do { WAIT_LGKM(); __builtin_amdgcn_s_barrier(); asm volatile("" ::: "memory"); } while (0)
#define GLD4(p) (G_NT ? __builtin_nontemporal_load(reinterpret_cast<const f32x4*>(p)) : *reinterpret_cast<const f32x4*>(p))
#define PIN() __builtin_amdgcn_sched_barrier(0)

__device__ __forceinline__ void lds_wait(unsigned* c, unsigned target) {
  while (__hip_atomic_load(c, RLX_WG) < target) __builtin_amdgcn_s_sleep(1);
  asm volatile("" ::: "memory");
}

__device__ __forceinline__ float merged_o(unsigned char* smem, int d) {
  f32x2 ml[NSW];
#pragma unroll
  for (int w = 0; w < NSW; ++w) ml[w] = *reinterpret_cast<const f32x2*>(LDS_F(L_ML) + 2 * w);
  float M = ml[0][0];
#pragma unroll
  for (int w = 1; w < NSW; ++w) M = fmaxf(M, ml[w][0]);
  float L = 0.f, o = 0.f;
#pragma unroll
  for (int w = 0; w < NSW; ++w) { const float sc = __builtin_amdgcn_exp2f(ml[w][0] - M); L = fmaf(sc, ml[w][1], L); o = fmaf(sc, LDS_F(L_MA)[w * 128 + d], o); }
  return o / L;
}

__device__ __forceinline__ float rbf(float x) { return bf_lo(pack_bf16(x, 0.f) & 0xffffu); }
__device__ __forceinline__ float half_sum(float x) {
  auto t = __builtin_amdgcn_permlane32_swap(__float_as_uint(x), __float_as_uint(x), false, false);
  return __uint_as_float(t[0]) + __uint_as_float(t[1]);
}
__device__ __forceinline__ void pack_b(const unsigned (&W)[32], u32x4 (&be)[4], u32x4 (&bo)[4]) {
#pragma unroll
  for (int s = 0; s < 4; ++s)
#pragma unroll
    for (int m = 0; m < 4; ++m) {
      be[s][m] = __builtin_amdgcn_perm(W[8 * s + 2 * m + 1], W[8 * s + 2 * m], 0x05040100u);
      bo[s][m] = __builtin_amdgcn_perm(W[8 * s + 2 * m + 1], W[8 * s + 2 * m], 0x07060302u);
    }
}
__device__ __forceinline__ void mfma_tile(const unsigned char* aimg, int lane, const u32x4 (&be)[4], const u32x4 (&bo)[4], f32x4& ae, f32x4& ao) {
#pragma unroll
  for (int s = 0; s < 4; ++s) {
    const bf16x8 av = *reinterpret_cast<const bf16x8*>(aimg + (lane & 15) * AROW + s * 64 + (lane >> 4) * 16);
    ae = __builtin_amdgcn_mfma_f32_16x16x32_bf16(av, __builtin_bit_cast(bf16x8, be[s]), ae, 0, 0, 0);
    ao = __builtin_amdgcn_mfma_f32_16x16x32_bf16(av, __builtin_bit_cast(bf16x8, bo[s]), ao, 0, 0, 0);
  }
}
__device__ __forceinline__ void fold_d(const f32x4& ae, const f32x4& ao, int lane, f32x2& v0, f32x2& v1) {
  float e[4], o[4];
#pragma unroll
  for (int i = 0; i < 4; ++i) { e[i] = half_sum(ae[i]); o[i] = half_sum(ao[i]); }
  const bool up = lane >= 32;
  v0 = f32x2{up ? e[2] : e[0], up ? o[2] : o[0]};
  v1 = f32x2{up ? e[3] : e[1], up ? o[3] : o[1]};
}

template <typename R>
__device__ __forceinline__ void poll4(u32x4 (&gi)[4], R rs, unsigned voff, unsigned soff, int far) {
#pragma unroll
  for (int k = 0; k < 4; ++k) gi[k] = u32x4{0u, 0u, 0u, 0u};
  for (unsigned spin = 0; spin < (1u << 17); ++spin) {
    bool miss[4];
#pragma unroll
    for (int k = 0; k < 4; ++k) miss[k] = (gi[k][0] == 0) | (gi[k][1] == 0) | (gi[k][2] == 0) | (gi[k][3] == 0);
    int nm = 0;
#pragma unroll
    for (int k = 0; k < 4; ++k) nm += __builtin_popcountll(__ballot(miss[k]));
    if (nm == 0) break;
    if (spin) { if (nm > far) __builtin_amdgcn_s_sleep(LSLEEP); else __builtin_amdgcn_s_sleep(1); }
#pragma unroll
    for (int k = 0; k < 4; ++k) if (miss[k]) gi[k] = __builtin_amdgcn_raw_buffer_load_b128(rs, voff, soff + k * 1024, 16);
    asm volatile("" ::: "memory");
  }
}

struct Args {
  const uint16_t* x; const uint16_t* kc; const uint16_t* vc; const uint16_t* wq; const uint16_t* wo;
  const uint16_t* w1; const uint16_t* b1; const uint16_t* w2; const uint16_t* b2; uint16_t* out; unsigned char* ws;
};

__device__ __forceinline__ void ln_pass(float (&v)[16], unsigned char* img, int row, int j, int lane) {
  float s = 0.f;
#pragma unroll
  for (int k = 0; k < 16; ++k) s += v[k];
  const float mean = wave_sum(s) * (1.f / DM);
  float q = 0.f;
#pragma unroll
  for (int k = 0; k < 16; ++k) { const float d = v[k] - mean; q += d * d; }
  const float var = wave_sum(q) * (1.f / DM);
  u32x4 hi[2], lo[2];
  const float mb = rbf(mean), inv = 1.f / rbf(sqrtf(rbf(rbf(var) + 1e-5f)));
#pragma unroll
  for (int k = 0; k < 8; ++k) {
    const unsigned d = pack_bf16(v[2 * k] - mb, v[2 * k + 1] - mb), e = pack_bf16(bf_lo(d) * inv, bf_hi(d) * inv);
    v[2 * k] = bf_lo(e); v[2 * k + 1] = bf_hi(e); hi[k >> 2][k & 3] = e; lo[k >> 2][k & 3] = 0u;
  }
  if ((lane >> 3) == j) {
    unsigned char* dst = img + row * AROW + (lane & 7) * 32;
    *reinterpret_cast<u32x4*>(dst) = hi[0]; *reinterpret_cast<u32x4*>(dst + 16) = hi[1];
    *reinterpret_cast<u32x4*>(dst + 8 * AROW) = lo[0]; *reinterpret_cast<u32x4*>(dst + 8 * AROW + 16) = lo[1];
  }
}
__device__ __forceinline__ void quarter_mm(const u32x4 (&W)[8], const unsigned char* img, int kq, float* redw, int lane) {
  const int n = lane & 15, g = lane >> 4;
  const bf16x8 av = *reinterpret_cast<const bf16x8*>(img + n * AROW + kq * 64 + g * 16);
  float d0[8], d1[8];
#pragma unroll
  for (int t = 0; t < 8; ++t) {
    u32x4 bf;
#pragma unroll
    for (int m = 0; m < 4; ++m) bf[m] = __builtin_amdgcn_perm(W[2 * m + 1][t >> 1], W[2 * m][t >> 1], (t & 1) ? 0x07060302u : 0x05040100u);
    const f32x4 d = __builtin_amdgcn_mfma_f32_16x16x32_bf16(av, __builtin_bit_cast(bf16x8, bf), f32x4{0.f, 0.f, 0.f, 0.f}, 0, 0, 0);
    auto p = __builtin_amdgcn_permlane32_swap(__float_as_uint(d[0]), __float_as_uint(d[2]), false, false);
    auto q = __builtin_amdgcn_permlane32_swap(__float_as_uint(d[1]), __float_as_uint(d[3]), false, false);
    d0[t] = __uint_as_float(p[0]) + __uint_as_float(p[1]);
    d1[t] = __uint_as_float(q[0]) + __uint_as_float(q[1]);
  }
  float* red = redw + (4 * (g & 1) + 2 * (g >> 1)) * 128 + 8 * n;
  *reinterpret_cast<f32x4*>(red) = f32x4{d0[0], d0[1], d0[2], d0[3]}; *reinterpret_cast<f32x4*>(red + 4) = f32x4{d0[4], d0[5], d0[6], d0[7]};
  *reinterpret_cast<f32x4*>(red + 128) = f32x4{d1[0], d1[1], d1[2], d1[3]}; *reinterpret_cast<f32x4*>(red + 132) = f32x4{d1[4], d1[5], d1[6], d1[7]};
}
#define WSYNC(i) do { WAIT_LGKM(); if (lane == 0) __hip_atomic_fetch_add(LDS_U(L_CNT) + (i), 1u, RLX_WG); lds_wait(LDS_U(L_CNT) + (i), 4); } while (0)

__global__ void __launch_bounds__(NTHR) attn_kernel(Args a) {
  __shared__ __attribute__((aligned(16))) unsigned char smem[L_END];
  const int tid = threadIdx.x, lane = tid & 63, wave_id = __builtin_amdgcn_readfirstlane(tid >> 6);
  const int r = lane >> 4, c = lane & 15;
  const int blk = blockIdx.x;
  const int h = (blk & 7) * 4 + (blk >> 6), b = (blk >> 3) & 7;
  const int me = b * NH + h;
  const auto rws = __builtin_amdgcn_make_buffer_rsrc((void*)a.ws, 0, WS_HAND, 0x00020000);
  if (wave_id >= 4) {
    const int wave = wave_id - 4;
    const uint16_t* kbase = a.kc + (size_t)me * SK * DH;
    const uint16_t* vbase = a.vc + (size_t)me * SK * DH;
    const auto rk = __builtin_amdgcn_make_buffer_rsrc((void*)kbase, 0, SK * DH * 2, 0x00020000);
    const unsigned voff = lane * 16;
    const unsigned t0 = (unsigned)__builtin_amdgcn_s_memrealtime() - (unsigned)(wave * 8 + b) * STAG;
    BARRIER();
    u32x4 KV[NBUF][16];
#define TOFF(j) ((unsigned)((j) * NSW + wave) * 16384u)
#define LDN(t) __builtin_amdgcn_raw_buffer_load_b128(rn_, voff + ((t) & 3) * 1024, so_ + ((t) >> 2) * 4096, KV_AUX)
#pragma unroll
    for (int g = 0; g < NBUF; ++g) { const auto rn_ = rk; const unsigned so_ = TOFF(g);
#pragma unroll
      for (int t = 0; t < 16; ++t) { KV[g][t] = LDN(t); PIN();
        if (g * 16 + t == PRE - 1) { lds_wait(LDS_U(L_CNT) + RESUME, 4); PIN(); }
      } }
    lds_wait(LDS_U(L_CNT) + 4, 4);
    unsigned qh[4], ql[4];
    {
      float q[8];
#pragma unroll
      for (int k = 0; k < 8; ++k) q[k] = xrow16_sum(LDS_F(L_QS)[r * 128 + 8 * c + k]);
      constexpr float C2 = MIMIC ? 1.f : 0.088388347648318440f * 1.4426950408889634f;
#pragma unroll
      for (int k = 0; k < 4; ++k) {
        const float q0 = q[2 * k] * C2, q1 = q[2 * k + 1] * C2;
        qh[k] = pack_bf16(q0, q1);
        ql[k] = pack_bf16(q0 - bf_lo(qh[k]), q1 - bf_hi(qh[k]));
      }
    }
#define PACE(S) do { if (TAU) { _Pragma("nounroll") for (int i_ = 0; i_ < 4096 && (unsigned)__builtin_amdgcn_s_memrealtime() - t0 < (unsigned)(S); ++i_) __builtin_amdgcn_s_sleep(4); PIN(); } } while (0)
    float* ps = LDS_F(L_P) + wave * (NS * 64);
    float mx = -INFINITY;
    for (int s0 = 0; s0 < NS; s0 += NBUF) {
#pragma unroll
      for (int g = 0; g < NBUF; ++g) {
        const int s_ = s0 + g, n_ = s_ + NBUF, nt_ = n_ < NS ? n_ : n_ - NS;
        const auto rn_ = __builtin_amdgcn_make_buffer_rsrc((void*)(n_ < NS ? kbase : vbase), 0, nt_ < NT ? SK * DH * 2 : 0, 0x00020000);
        const unsigned so_ = TOFF(nt_ & (NT - 1));
        float x_[16];
        PACE(T0 + s_ * TAU);
#pragma unroll
        for (int t = 0; t < 16; ++t) { float d_ = 0.f;
#pragma unroll
          for (int k = 0; k < 4; ++k) d_ = dot2(KV[g][t][k], qh[k], d_);
          asm volatile("" : "+v"(d_)); x_[t] = d_; PIN(); KV[g][t] = LDN(t); PIN(); }
#pragma unroll
        for (int i = 0; i < 8; ++i) { const float e_ = (c & 8) ? x_[i] : x_[i + 8], k_ = (c & 8) ? x_[i + 8] : x_[i]; x_[i] = k_ + dpp<XOR8>(e_); }
#pragma unroll
        for (int i = 0; i < 4; ++i) { const float e_ = (c & 4) ? x_[i] : x_[i + 4], k_ = (c & 4) ? x_[i + 4] : x_[i]; x_[i] = k_ + dpp<XOR7>(e_); }
#pragma unroll
        for (int i = 0; i < 2; ++i) { const float e_ = (c & 2) ? x_[i] : x_[i + 2], k_ = (c & 2) ? x_[i + 2] : x_[i]; x_[i] = k_ + dpp<XOR2>(e_); }
        { const float e_ = (c & 1) ? x_[0] : x_[1], k_ = (c & 1) ? x_[1] : x_[0]; x_[0] = k_ + dpp<XOR1>(e_); }
        x_[0] = rbf(rbf(x_[0]) * 0.08838834764831845f) * 1.4426950408889634f;
        mx = fmaxf(mx, (NS == NT || s_ < NT) ? x_[0] : -INFINITY); ps[s_ * 64 + lane] = x_[0]; PIN();
      }
    }
    const float m = wave_max(mx);
    float l = 0.f;
#pragma unroll
    for (int j = 0; j < NT; ++j) { const float p = __builtin_amdgcn_exp2f(ps[j * 64 + lane] - m); l += p; ps[j * 64 + lane] = p; }
    l = wave_sum(l);
    f32x2 acc[4];
#pragma unroll
    for (int k = 0; k < 4; ++k) acc[k] = f32x2{0.f, 0.f};
    PIN();
    for (int s0 = 0; s0 < NS; s0 += NBUF) {
#pragma unroll
      for (int g = 0; g < NBUF; ++g) {
        const int s_ = s0 + g, n_ = s_ + NBUF;
        const auto rn_ = __builtin_amdgcn_make_buffer_rsrc((void*)vbase, 0, n_ < NT ? SK * DH * 2 : 0, 0x00020000);
        const unsigned so_ = TOFF(n_ & (NT - 1));
        f32x4 p_[4];
        PACE(n_ < NT ? T0 + NS * TAU + s_ * TAUV : 0);
#pragma unroll
        for (int u = 0; u < 4; ++u) p_[u] = *reinterpret_cast<const f32x4*>(ps + s_ * 64 + r * 16 + u * 4);
#pragma unroll
        for (int t = 0; t < 16; ++t) { fma8(acc, p_[t >> 2][t & 3], KV[g][t]); PIN(); KV[g][t] = LDN(t); PIN(); }
      }
    }
    float o8[8];
#pragma unroll
    for (int k = 0; k < 4; ++k) { o8[2 * k] = xrow16_sum(acc[k][0]); o8[2 * k + 1] = xrow16_sum(acc[k][1]); }
    if (r == 0) {
      *reinterpret_cast<f32x4*>(LDS_F(L_MA) + wave * 128 + 8 * c) = f32x4{o8[0], o8[1], o8[2], o8[3]};
      *reinterpret_cast<f32x4*>(LDS_F(L_MA) + wave * 128 + 8 * c + 4) = f32x4{o8[4], o8[5], o8[6], o8[7]};
      if (c == 0) *reinterpret_cast<f32x2*>(LDS_F(L_ML) + wave * 2) = f32x2{m, l};
    }
    BARRIER();
    if (wave != 0) return;
    const int ob = h >> 2, col = b * 128 + (h & 3) * 32 + (lane & 7) * 4;
    const u32x2 xv = *reinterpret_cast<const u32x2*>(a.x + ob * DM + col), bv = *reinterpret_cast<const u32x2*>(a.b2 + col);
    u32x4 gi[4];
    if (W2) lds_wait(LDS_U(L_CNT) + 1, 1);
    poll4(gi, rws, lane * 16, WS_MSG + me * 4096, SLEEP2);
    float sv[4];
#pragma unroll
    for (int e = 0; e < 4; ++e) {
      float t = (__uint_as_float(gi[0][e]) + __uint_as_float(gi[1][e])) + (__uint_as_float(gi[2][e]) + __uint_as_float(gi[3][e]));
      t += dpp<XOR8>(t); sv[e] = xrow16_sum(t);
    }
    if (lane < 8) *reinterpret_cast<u32x2*>(a.out + ob * DM + col) =
        u32x2{pack_bf16(bf_lo(xv[0]) + sv[0] + bf_lo(bv[0]), bf_hi(xv[0]) + sv[1] + bf_hi(bv[0])), pack_bf16(bf_lo(xv[1]) + sv[2] + bf_lo(bv[1]), bf_hi(xv[1]) + sv[3] + bf_hi(bv[1]))};
  } else {
    const int u = wave_id, t = tid;
    __builtin_amdgcn_s_setprio(3);
    gu32* nonces = (gu32*)(a.ws + WS_NON);
    gu64* hel_in = (gu64*)(a.ws + WS_HEL) + me * 64;
    const int slot = ((h >> 2) & 1) * 16 + (h >> 3) * 4 + (h & 3);
    u32x4 xr[4];
#pragma unroll
    for (int k = 0; k < 4; ++k) xr[k] = *reinterpret_cast<const u32x4*>(a.x + (2 * u + (k >> 1)) * DM + lane * 16 + (k & 1) * 8);
    PIN();
    u32x4 W[8];
    {
      const auto rw = __builtin_amdgcn_make_buffer_rsrc((void*)(a.wq + (size_t)(b * 128 + u * 32) * (NH * DH) + h * DH), 0, 32 * NH * DH * 2, 0x00020000);
#pragma unroll
      for (int e = 0; e < 8; ++e) W[e] = __builtin_amdgcn_raw_buffer_load_b128(rw, (unsigned)(8 * r) * (NH * DH * 2) + c * 16, e * (NH * DH * 2), W_AUX);
    }
    PIN();
    const unsigned old = __builtin_amdgcn_raw_ptr_buffer_atomic_add_i32(1, rws, t == 192 ? WS_NON + me * 128 : 0xfffffff0u, 0, 0);
    const u32x4 sc0 = __builtin_amdgcn_raw_buffer_load_b128(rws, lane * 16, WS_QIN + me * 8192 + u * 2048, 16);
    const u32x4 sc1 = __builtin_amdgcn_raw_buffer_load_b128(rws, lane * 16, WS_QIN + me * 8192 + u * 2048 + 1024, 16);
    const unsigned htag = __builtin_amdgcn_raw_buffer_load_b32(rws, lane * 8 + 4, WS_HEL + me * 512, 16);
    PIN();
    if (t < 16) LDS_U(L_CNT)[t] = 0;
    if (u == 1) LDS_U(L_BM)[lane] = 0;
    PIN();
    BARRIER();
    __builtin_amdgcn_s_sleep(NDELAY);
    const int pb = 2 * u + (lane >> 5), partner = pb * NH + h;
    unsigned used = __hip_atomic_load(nonces + partner * 32, RLX_AGENT);
    PIN();
    float va[16], vb[16];
#pragma unroll
    for (int k = 0; k < 8; ++k) { va[2 * k] = bf_lo(xr[k >> 2][k & 3]); va[2 * k + 1] = bf_hi(xr[k >> 2][k & 3]); vb[2 * k] = bf_lo(xr[2 + (k >> 2)][k & 3]); vb[2 * k + 1] = bf_hi(xr[2 + (k >> 2)][k & 3]); }
    ln_pass(va, smem + L_AX, 2 * u, b, lane);
    ln_pass(vb, smem + L_AX, 2 * u + 1, b, lane);
    if (t == 192) LDS_U(L_CNT)[8] = old + 1u;
    WSYNC(2);
    quarter_mm(W, smem + L_AX, u, LDS_F(L_RED) + u * 1024, lane);
    const unsigned n0 = LDS_U(L_CNT)[8];
    {
      asm volatile("" :: "v"(sc0), "v"(sc1));
      const unsigned tg[5] = {sc0[1] - n0, sc0[3] - n0, sc1[1] - n0, sc1[3] - n0, htag - n0};
#pragma unroll
      for (int k = 0; k < 5; ++k) if (tg[k] < 2048u) __hip_atomic_fetch_or(LDS_U(L_BM) + (tg[k] >> 5), 1u << (tg[k] & 31), RLX_WG);
    }
    WSYNC(3);
    unsigned nonce;
    {
      const unsigned fr = ~LDS_U(L_BM)[lane];
      const int w0 = __builtin_ctzll(__ballot(fr != 0u));
      nonce = n0 + w0 * 32 + __builtin_ctz(__builtin_amdgcn_readlane(fr, w0));
      if (nonce != n0 && t == 192) __hip_atomic_store(nonces + me * 32, nonce, RLX_AGENT);
    }
    {
      const f32x4* red = reinterpret_cast<const f32x4*>(smem + L_RED) + t;
      const f32x4 v = (red[0] + red[256]) + (red[512] + red[768]);
      const unsigned qdst = WS_QIN + (unsigned)partner * 8192 + b * 1024 + (lane & 31) * 32;
      gu64* hel_out = (gu64*)(a.ws + WS_HEL) + partner * 64 + 32 + b;
#define SENDQ() do { \
        __builtin_amdgcn_raw_buffer_store_b128(u32x4{__float_as_uint(v[0]), used, __float_as_uint(v[1]), used}, rws, qdst, 0, 16); \
        __builtin_amdgcn_raw_buffer_store_b128(u32x4{__float_as_uint(v[2]), used, __float_as_uint(v[3]), used}, rws, qdst + 16, 0, 16); \
        if ((lane & 31) == 0) __hip_atomic_store(hel_out, ((unsigned long long)used << 32) | nonce, RLX_AGENT); } while (0)
      SENDQ();
      u32x4 g0, g1;
      for (unsigned spin = 0; spin < (1u << 17); ++spin) {
        g0 = __builtin_amdgcn_raw_buffer_load_b128(rws, lane * 16, WS_QIN + me * 8192 + u * 2048, 16);
        g1 = __builtin_amdgcn_raw_buffer_load_b128(rws, lane * 16, WS_QIN + me * 8192 + u * 2048 + 1024, 16);
        const unsigned long long hv = __hip_atomic_load(hel_in + 32 + pb, RLX_AGENT);
        const unsigned cur = __hip_atomic_load(nonces + partner * 32, RLX_AGENT);
        const bool hok = (unsigned)(hv >> 32) == nonce;
        const unsigned now = hok ? (unsigned)hv : cur;
        if (now != used) { used = now; SENDQ(); }
        if (__all(hok & (g0[1] == nonce) & (g0[3] == nonce) & (g1[1] == nonce) & (g1[3] == nonce))) break;
        asm volatile("" ::: "memory");
      }
      *reinterpret_cast<f32x2*>(LDS_F(L_QS) + u * 128 + 2 * lane) = f32x2{__uint_as_float(g0[0]) + __uint_as_float(g1[0]), __uint_as_float(g0[2]) + __uint_as_float(g1[2])};
      WSYNC(4);
    }
    __builtin_amdgcn_s_setprio(1);
    {
      const u32x4 zero4 = {0u, 0u, 0u, 0u};
      __builtin_amdgcn_raw_buffer_store_b128(zero4, rws, lane * 16, WS_HO + (h * 8 + b) * 4096 + u * 1024, 16);
      __builtin_amdgcn_raw_buffer_store_b128(zero4, rws, (unsigned)(lane >> 3) * 4096 + (lane & 7) * 16, WS_MSG + (b * NH + 8 * u) * 4096 + slot * 128, 16);
      __builtin_amdgcn_raw_buffer_store_b128(zero4, rws, lane < 8 ? WS_OX + (h * 8 + b) * 512 + u * 128 + lane * 16 : 0xfffffff0u, 0, 16);
    }
    {
      const auto rw = __builtin_amdgcn_make_buffer_rsrc((void*)(a.w1 + (size_t)(b * 128 + u * 32) * FF + h * FS), 0, 32 * FF * 2, 0x00020000);
#pragma unroll
      for (int e = 0; e < 8; ++e) W[e] = __builtin_amdgcn_raw_buffer_load_b128(rw, (unsigned)(8 * r) * (FF * 2) + (c > 13 ? 13 : c) * 16, e * (FF * 2), W_AUX);
      ln_pass(va, smem + L_AX + 16 * AROW, 2 * u, b, lane);
      ln_pass(vb, smem + L_AX + 16 * AROW, 2 * u + 1, b, lane);
      WAIT_VM();
      WSYNC(6);
      quarter_mm(W, smem + L_AX + 16 * AROW, u, LDS_F(L_RED) + u * 1024, lane);
      WSYNC(5);
      const f32x4* red = reinterpret_cast<const f32x4*>(smem + L_RED) + t;
      const f32x4 v = (red[0] + red[256]) + (red[512] + red[768]);
      __builtin_amdgcn_raw_buffer_store_b128(u32x4{__float_as_uint(v[0]) | 1u, __float_as_uint(v[1]) | 1u, __float_as_uint(v[2]) | 1u, __float_as_uint(v[3]) | 1u},
                                             rws, t * 16, WS_HO + (h * 8 + b) * 4096, 16);
    }
    if (u == 3) {
      const bool colg = lane < 32;
      const int jw = colg ? b * NH + lane : (lane & 7) * NH + h;
      gu64* hel_out = (gu64*)(a.ws + WS_HEL) + jw * 64 + (colg ? h : 40 + b);
      unsigned us = __hip_atomic_load(nonces + jw * 32, RLX_AGENT);
      __hip_atomic_store(hel_out, ((unsigned long long)us << 32) | nonce, RLX_AGENT);
      for (unsigned spin = 0; spin < (1u << 17); ++spin) {
        const unsigned long long hv = __hip_atomic_load(hel_in + (colg ? lane : 40 + (lane & 7)), RLX_AGENT);
        const unsigned cur = __hip_atomic_load(nonces + jw * 32, RLX_AGENT);
        const bool ok = (unsigned)(hv >> 32) == nonce;
        const unsigned now = ok ? (unsigned)hv : cur;
        if (now != us) { us = now; __hip_atomic_store(hel_out, ((unsigned long long)us << 32) | nonce, RLX_AGENT); }
        if (__all(ok)) break;
        __builtin_amdgcn_s_sleep(32);
      }
    }
    WSYNC(7);
    {
      const int tt = t < 224 ? t : 0, hb = tt / 28, f4 = (tt % 28) * 4;
      f32x4 z[8];
      for (unsigned spin = 0; spin < (1u << 17); ++spin) {
        bool ok = true;
#pragma unroll
        for (int k = 0; k < 8; ++k) { const u32x4 zz = __builtin_amdgcn_raw_buffer_load_b128(rws, (unsigned)(hb * 128 + f4) * 4, WS_HO + (h * 8 + k) * 4096, 16);
          ok &= (zz[0] != 0) & (zz[1] != 0) & (zz[2] != 0) & (zz[3] != 0); z[k] = __builtin_bit_cast(f32x4, zz); }
        if (__all(ok)) break;
        __builtin_amdgcn_s_sleep(32);
        asm volatile("" ::: "memory");
      }
      const u32x2 bb = *reinterpret_cast<const u32x2*>(a.b1 + h * FS + f4);
      const f32x4 zs = ((z[0] + z[1]) + (z[2] + z[3])) + ((z[4] + z[5]) + (z[6] + z[7])) + f32x4{bf_lo(bb[0]), bf_hi(bb[0]), bf_lo(bb[1]), bf_hi(bb[1])};
      float ge[4];
#pragma unroll
      for (int k = 0; k < 4; ++k) { const float v = zs[k]; ge[k] = 0.5f * v * (1.f + tanhf(0.7978845608028654f * (v + 0.044715f * v * v * v))); }
      const unsigned h0 = pack_bf16(ge[0], ge[1]), h1 = pack_bf16(ge[2], ge[3]);
      if (t < 224) {
        *reinterpret_cast<u32x2*>(smem + L_AH + hb * AROW + f4 * 2) = u32x2{h0, h1};
        *reinterpret_cast<u32x2*>(smem + L_AH + (8 + hb) * AROW + f4 * 2) = u32x2{pack_bf16(ge[0] - bf_lo(h0), ge[1] - bf_hi(h0)), pack_bf16(ge[2] - bf_lo(h1), ge[3] - bf_hi(h1))};
      } else {
        *reinterpret_cast<u32x4*>(smem + L_AH + ((t - 224) >> 1) * AROW + 224 + (t & 1) * 16) = u32x4{0u, 0u, 0u, 0u};
      }
    }
    {
      const uint16_t* s2 = a.w2 + ((size_t)h * FS + 28 * u + r) * DM + b * 128 + c * 8;
      const uint16_t* so = a.wo + ((size_t)h * DH + 32 * u + r) * DM + b * 128 + c * 8;
#pragma unroll
      for (int i = 0; i < 7; ++i) __builtin_amdgcn_global_load_lds((glb_ptr_t)(s2 + i * 4 * DM), (lds_ptr_t)(smem + L_W2 + (7 * u + i) * 1024), 16, 0, W_AUX);
#pragma unroll
      for (int i = 0; i < 8; ++i) __builtin_amdgcn_global_load_lds((glb_ptr_t)(so + i * 4 * DM), (lds_ptr_t)(smem + L_WO + (8 * u + i) * 1024), 16, 0, W_AUX);
      WAIT_VM();
      WSYNC(0);
    }
    u32x4 be[4], bo[4];
    f32x4 ae = {0.f, 0.f, 0.f, 0.f}, ao = {0.f, 0.f, 0.f, 0.f};
    {
      unsigned Wt[32];
#pragma unroll
      for (int i = 0; i < 32; ++i) Wt[i] = *reinterpret_cast<const unsigned*>(smem + L_W2 + ((i >> 3) * 32 + (i & 7) + 8 * (i < 24 ? r : (r & 1))) * 256 + u * 64 + c * 4);
      pack_b(Wt, be, bo);
      mfma_tile(smem + L_AH, lane, be, bo, ae, ao);
    }
    {
      unsigned Wt[32];
#pragma unroll
      for (int i = 0; i < 32; ++i) Wt[i] = *reinterpret_cast<const unsigned*>(smem + L_WO + ((i >> 3) * 32 + (i & 7) + 8 * r) * 256 + u * 64 + c * 4);
      pack_b(Wt, be, bo);
#pragma unroll
      for (int s = 0; s < 4; ++s) asm volatile("" : "+v"(be[s]), "+v"(bo[s]));
    }
    BARRIER();
    if (u == 0) {
      const float o0 = merged_o(smem, 2 * lane), o1 = merged_o(smem, 2 * lane + 1);
      const unsigned hi = pack_bf16(o0, o1), lo = MIMIC ? 0u : pack_bf16(o0 - bf_lo(hi), o1 - bf_hi(hi));
      const u32x2 wv = {(hi << 16) | (lo & 0xffffu) | 1u, (hi & 0xffff0000u) | (lo >> 16) | 1u};
      __builtin_amdgcn_raw_buffer_store_b64(wv, rws, lane * 8, WS_OX + (h * 8 + b) * 512, 16);
      u32x4 gi[4];
      poll4(gi, rws, lane * 16, WS_OX + h * 4096, SLEEP1);
#pragma unroll
      for (int k = 0; k < 4; ++k) {
        unsigned char* dst = smem + L_AO + (2 * k + (lane >> 5)) * AROW + (lane & 31) * 8;
        *reinterpret_cast<u32x2*>(dst) = u32x2{__builtin_amdgcn_perm(gi[k][1], gi[k][0], 0x07060302u), __builtin_amdgcn_perm(gi[k][3], gi[k][2], 0x07060302u)};
        *reinterpret_cast<u32x2*>(dst + 8 * AROW) = u32x2{__builtin_amdgcn_perm(gi[k][1], gi[k][0], 0x05040100u) & 0xfffefffeu, __builtin_amdgcn_perm(gi[k][3], gi[k][2], 0x05040100u) & 0xfffefffeu};
      }
      WAIT_LGKM();
      if (lane == 0) __hip_atomic_store(LDS_U(L_CNT) + 1, 1u, RLX_WG);
    } else {
      lds_wait(LDS_U(L_CNT) + 1, 1);
    }
    mfma_tile(smem + L_AO, lane, be, bo, ae, ao);
    f32x2 v0, v1;
    fold_d(ae, ao, lane, v0, v1);
    const int b0 = 4 * (r & 1) + 2 * (r >> 1);
    const unsigned dst = WS_MSG + (unsigned)(b * NH + b0 * 4 + u) * 4096 + slot * 128 + c * 8;
    __builtin_amdgcn_raw_buffer_store_b64(u32x2{__float_as_uint(v0[0]) | 1u, __float_as_uint(v0[1]) | 1u}, rws, dst, 0, 16);
    __builtin_amdgcn_raw_buffer_store_b64(u32x2{__float_as_uint(v1[0]) | 1u, __float_as_uint(v1[1]) | 1u}, rws, dst + 4 * 4096, 0, 16);
  }
}

extern "C" void kernel_launch(void* const* d_in, const int* in_sizes, int n_in, void* d_out, int out_size, void* d_ws, size_t ws_size,
                              hipStream_t stream) {
  Args a;
  a.x = (const uint16_t*)d_in[0]; a.kc = (const uint16_t*)d_in[1]; a.vc = (const uint16_t*)d_in[2];
  a.wq = (const uint16_t*)d_in[3]; a.wo = (const uint16_t*)d_in[4];
  a.w1 = (const uint16_t*)d_in[6]; a.b1 = (const uint16_t*)d_in[7]; a.w2 = (const uint16_t*)d_in[8]; a.b2 = (const uint16_t*)d_in[9];
  a.out = (uint16_t*)d_out; a.ws = (unsigned char*)d_ws;
  hipLaunchKernelGGL(attn_kernel, dim3(NB_ * NH), dim3(NTHR), 0, stream, a);
}
```
